```python
import torch

def _softmax(x, axis=-1):
    return torch.softmax(x, dim=axis)
def _gelu(x):
    return torch.nn.functional.gelu(x, approximate="tanh")
def _silu(x):
    return torch.nn.functional.silu(x)
def _sigmoid(x):
    return torch.sigmoid(x)
def _layernorm(x, axis=-1, eps=1e-5):
    m = x.mean(dim=axis, keepdim=True)
    v = x.var(dim=axis, keepdim=True, unbiased=False)
    return (x - m) / torch.sqrt(v + eps)
def _scatter_add(tgt, idx, src, axis=0):
    return tgt.clone().index_add_(axis, idx, src)
def _segment_sum(src, seg, n):
    out = torch.zeros((n,) + src.shape[1:], dtype=src.dtype)
    return out.index_add_(0, seg, src)
def _pad(x, pw):
    flat = tuple(v for pair in reversed(pw) for v in pair)
    return torch.nn.functional.pad(x, flat)

kernel_name = 'sequential_preLN__mha_block__dense_activation_squared_relu__prompt'

def setup_inputs():

    _w = (lambda a: (lambda t: t.to(torch.bfloat16) if t.is_floating_point() else t)(torch.as_tensor(a)))
    def _A_setup():
        B, S, D, H = 1, 2048, 640, 32
        return {
            "x": _w((lambda *s: torch.randn(*s, dtype=torch.bfloat16) * 0.05)(B, S, D)),
            "wqkv": _w((lambda *s: torch.randn(*s, dtype=torch.bfloat16) * 0.05)(D, 3 * D)),
            "wo": _w((lambda *s: torch.randn(*s, dtype=torch.bfloat16) * 0.05)(D, D)),
        }
    _A_in = _A_setup()

    def _B_setup():
        B, S, D, F = 1, 2048, 640, 1536
        return {
            "x": _w((lambda *s: torch.randn(*s, dtype=torch.bfloat16) * 0.05)(B, S, D)),
            "w1": _w((lambda *s: torch.randn(*s, dtype=torch.bfloat16) * 0.05)(D, F)),
            "b1": _w((lambda *s: torch.randn(*s, dtype=torch.bfloat16) * 0.8)(F)),
            "w2": _w((lambda *s: torch.randn(*s, dtype=torch.bfloat16) * 0.05)(F, D)),
            "b2": _w((lambda *s: torch.randn(*s, dtype=torch.bfloat16) * 3.2)(D)),
        }
    _B_in = _B_setup()

    return {
        "A_x": _A_in["x"],
        "A_wqkv": _A_in["wqkv"],
        "A_wo": _A_in["wo"],
        "B_x": _B_in["x"],
        "B_w1": _B_in["w1"],
        "B_b1": _B_in["b1"],
        "B_w2": _B_in["w2"],
        "B_b2": _B_in["b2"],
    }

def reference(A_x, A_wqkv, A_wo, B_x, B_w1, B_b1, B_w2, B_b2):
    def _A_ref(x, wqkv, wo):
        B, S, D = x.shape
        H = 32
        Dh = D // H
        qkv = (lambda x, *d: x.permute(*d))((x @ wqkv).reshape(B, S, 3, H, Dh), 2, 0, 3, 1, 4)
        q, k, v = qkv[0], qkv[1], qkv[2]
        scores = torch.einsum("bhqd,bhkd->bhqk", q, k) * (Dh ** -0.5)
        p = _softmax(scores, axis=-1)
        o = torch.einsum("bhqk,bhkd->bhqd", p, v)
        return (lambda x, *d: x.permute(*d))(o, 0, 2, 1, 3).reshape(B, S, D) @ wo

    def _B_ref(x, w1, b1, w2, b2):
        h = torch.einsum("bsd,df->bsf", x, w1) + b1
        zero = torch.zeros_like(h)
        r = torch.where(h > zero, h, zero)
        h = r * r
        out = torch.einsum("bsf,fd->bsd", h, w2) + b2
        return out

    x = A_x
    h = x + _A_ref(_layernorm(x, axis=-1), A_wqkv, A_wo)
    h2 = h + _B_ref(_layernorm(h, axis=-1), B_w1, B_b1, B_w2, B_b2)
    return h2
```

```cpp
#include <hip/hip_runtime.h>
#include <hip/hip_bf16.h>
#include <stdint.h>
#include <type_traits>
#include <utility>

typedef __attribute__((ext_vector_type(8))) __bf16 bh8;
typedef __attribute__((ext_vector_type(4))) __bf16 bh4;
typedef __attribute__((ext_vector_type(2))) __bf16 bh2;
typedef __attribute__((ext_vector_type(4))) short s4v;
typedef __attribute__((ext_vector_type(8))) short s8v;
typedef __attribute__((ext_vector_type(4))) float f4;
typedef __attribute__((ext_vector_type(2))) float f2;
typedef __attribute__((ext_vector_type(16))) float f16v;
typedef __attribute__((ext_vector_type(4))) unsigned u4;
typedef __attribute__((ext_vector_type(2))) unsigned u2;
#define LAS __attribute__((address_space(3)))
#define DEVI __device__ __forceinline__

constexpr int S_ = 2048, D_ = 640, F_ = 1536;
constexpr int KV_TILE = 2816;
constexpr size_t OFF_Q = 0;
constexpr size_t OFF_KV = OFF_Q + (size_t)32 * 2048 * 40;
constexpr size_t OFF_GMAX = OFF_KV + (size_t)32 * 64 * KV_TILE;
constexpr size_t OFF_O = OFF_GMAX + 40960;
constexpr size_t OFF_H = OFF_O + (size_t)128 * 20 * 1024;
constexpr size_t OFF_ST = OFF_H + (size_t)128 * 20 * 1024;
constexpr size_t OFF_H1 = OFF_ST + (size_t)8 * 2048 * 8;
constexpr size_t OFF_WO = OFF_H1 + (size_t)128 * 48 * 1024;
constexpr size_t OFF_W1 = OFF_WO + (size_t)40 * 20 * 1024;
constexpr size_t OFF_W2 = OFF_W1 + (size_t)96 * 20 * 1024;

constexpr size_t OFF_CNT = OFF_W2 + (size_t)40 * 48 * 1024;

DEVI void glds16(const void* sbase, unsigned voff, unsigned lds_addr) {
  asm volatile("s_mov_b32 m0, %2\n\ts_nop 0\n\tglobal_load_lds_dwordx4 %0, %1" ::"v"(voff), "s"(sbase), "s"(lds_addr) : "memory");
}
template <int IMM>
DEVI void gload16(bh8& dst, const void* sbase, unsigned voff) {
  asm volatile("global_load_dwordx4 %0, %1, %2 offset:%3" : "=v"(dst) : "v"(voff), "s"(sbase), "i"(IMM) : "memory");
}
template <int IMM>
DEVI void gload8(u2& dst, const void* sbase, unsigned voff) {
  asm volatile("global_load_dwordx2 %0, %1, %2 offset:%3" : "=v"(dst) : "v"(voff), "s"(sbase), "i"(IMM) : "memory");
}
template <int IMM>
DEVI void gload4(unsigned& dst, const void* sbase, unsigned voff) {
  asm volatile("global_load_dword %0, %1, %2 offset:%3" : "=v"(dst) : "v"(voff), "s"(sbase), "i"(IMM) : "memory");
}
DEVI void st16(char* p, u4 v) { asm volatile("global_store_dwordx4 %0, %1, off sc1" ::"v"(p), "v"(v) : "memory"); }
template <int IMM>
DEVI void st16s(char* sb, unsigned vo, u4 v) { asm volatile("global_store_dwordx4 %0, %1, %2 offset:%3 sc1" ::"v"(vo), "v"(v), "s"(sb), "i"(IMM) : "memory"); }
DEVI void st8s(char* sb, unsigned vo, u2 v) { asm volatile("global_store_dwordx2 %0, %1, %2 sc1" ::"v"(vo), "v"(v), "s"(sb) : "memory"); }
DEVI void st8(char* p, u2 v) { asm volatile("global_store_dwordx2 %0, %1, off sc1" ::"v"(p), "v"(v) : "memory"); }
template <int N>
DEVI void wait_vm_barrier() {
  asm volatile("s_waitcnt vmcnt(%0) lgkmcnt(0)\n\ts_barrier" ::"i"(N) : "memory");
}
template <int N>
DEVI void wait_vm() { asm volatile("s_waitcnt vmcnt(%0)" ::"i"(N) : "memory"); }
DEVI void pin(bh8& a) { asm volatile("" : "+v"(a)); }
DEVI void pin(u2& a) { asm volatile("" : "+v"(a)); }
DEVI bh8 ldsA(unsigned addr0, unsigned addr1) {
  s4v x = __builtin_amdgcn_ds_read_tr16_b64_v4i16((LAS s4v*)addr0);
  s4v y = __builtin_amdgcn_ds_read_tr16_b64_v4i16((LAS s4v*)addr1);
  s8v z = __builtin_shufflevector(x, y, 0, 1, 2, 3, 4, 5, 6, 7);
  return __builtin_bit_cast(bh8, z);
}
DEVI f4 mfma16(bh8 a, bh8 b, f4 c) { return __builtin_amdgcn_mfma_f32_16x16x32_bf16(a, b, c, 0, 0, 0); }
DEVI unsigned pk2(float a, float b) { bh2 r = {(__bf16)a, (__bf16)b}; return __builtin_bit_cast(unsigned, r); }
DEVI float asf(unsigned u) { return __builtin_bit_cast(float, u); }
DEVI float lo16(unsigned u) { return __builtin_bit_cast(float, u << 16); }
DEVI float hi16(unsigned u) { return __builtin_bit_cast(float, u & 0xffff0000u); }
DEVI float xsum_g(float v) { v += __shfl_xor(v, 16); v += __shfl_xor(v, 32); return v; }
template <int CTRL>
DEVI float dppmax(float v) { return fmaxf(v, asf((unsigned)__builtin_amdgcn_update_dpp(0, (int)__builtin_bit_cast(unsigned, v), CTRL, 0xf, 0xf, true))); }
DEVI float max16(float v) { return dppmax<0x140>(dppmax<0x141>(dppmax<0x4e>(dppmax<0xb1>(v)))); }
template <int CTRL>
DEVI float dppadd(float v) { return v + asf((unsigned)__builtin_amdgcn_update_dpp(0, (int)__builtin_bit_cast(unsigned, v), CTRL, 0xf, 0xf, true)); }
DEVI float sum8(float v) { return dppadd<0x141>(dppadd<0x4e>(dppadd<0xb1>(v))); }
DEVI int uni(int v) { return __builtin_amdgcn_readfirstlane(v); }

#define IC(n) std::integral_constant<int, n>{}
template <class Fn, int... I>
DEVI void static_for_impl(Fn&& f, std::integer_sequence<int, I...>) { (f(std::integral_constant<int, I>{}), ...); }
template <int N, class Fn>
DEVI void static_for(Fn&& f) { static_for_impl(f, std::make_integer_sequence<int, N>{}); }

DEVI f16v mfma32(bh8 a, bh8 b, f16v c) { return __builtin_amdgcn_mfma_f32_32x32x16_bf16(a, b, c, 0, 0, 0); }
#define SGB(mask, n) __builtin_amdgcn_sched_group_barrier(mask, n, 0)
#define K1ROT ((nt & 7) + (9 * (mt & 3)) / 4)
#define NCOM 1
#define PRIO_AT 8
constexpr int CL0 = 186, CL1 = 186;
constexpr int K1_LDS = 131072 + 2048;
DEVI void bar() { asm volatile("s_barrier" ::: "memory"); }
DEVI void qkv_loader(const char* __restrict__ x, const char* __restrict__ wqkv, unsigned lds0, int mt, int nt, int w, int l) {
  const int i8 = l >> 3;
  const unsigned xv0 = (unsigned)(i8 * 1280 + (((l & 7) ^ (i8 >> 1)) << 4)), xv1 = xv0 ^ 64u;
  const unsigned wv = (unsigned)(i8 * 3840 + (((l & 7) ^ (((l >> 4) & 1) << 2)) << 4));
  const char* xb = x + (size_t)(mt * 128 + w * 32) * 1280;
  const char* wb = wqkv + (size_t)((w & 1) * 32) * 3840 + nt * 256 + (w >> 1) * 128;
  int kc = 0;
  auto stage = [&](int slot) {
    const char* xs = xb + kc * 128;
    const char* wsrc = wb + (size_t)kc * 64 * 3840;
    const unsigned d = lds0 + slot * 32768 + w * 4096;
    glds16(xs, xv0, d); glds16(wsrc, wv, d + 16384);
    glds16(xs + 8 * 1280, xv1, d + 1024); glds16(wsrc + 8 * 3840, wv, d + 16384 + 1024);
    glds16(xs + 16 * 1280, xv0, d + 2048); glds16(wsrc + 16 * 3840, wv, d + 16384 + 2048);
    glds16(xs + 24 * 1280, xv1, d + 3072); glds16(wsrc + 24 * 3840, wv, d + 16384 + 3072);
    kc = kc == 9 ? NCOM : kc + 1;
  };
  stage(0);
  if (NCOM == 0) kc = 1;
  if (NCOM == 1) kc = 1 + (K1ROT) % 9;
  stage(1);
  if (NCOM == 2) kc = 2 + (K1ROT) % 8;
  wait_vm_barrier<8>();
  stage(2);
#pragma unroll 1
  for (int i = 0; i < 7; ++i) { wait_vm_barrier<8>(); stage((i + 3) & 3); }
  wait_vm_barrier<8>();
  wait_vm_barrier<0>();
}
DEVI void acc2(float& s, float& t, float p, float q) {
  asm volatile("v_add_f32 %0, %0, %2\n\tv_fma_f32 %1, %2, %2, %1\n\tv_add_f32 %0, %0, %3\n\tv_fma_f32 %1, %3, %3, %1" : "+v"(s), "+v"(t) : "v"(p), "v"(q));
}
DEVI void qkv_stats(unsigned lds0, int w, int l) {
  float sx[4] = {0.f, 0.f, 0.f, 0.f}, sq[4] = {0.f, 0.f, 0.f, 0.f};
  u4 v[2][4];
  static_for<11>([&](auto ic) {
    constexpr int i = decltype(ic)::value;
    if constexpr (i < 10) {
      asm volatile("s_waitcnt lgkmcnt(0)\n\ts_barrier" ::: "memory");
      if constexpr (i == PRIO_AT) __builtin_amdgcn_s_setprio(3);
      const unsigned a = lds0 + (i & 3) * 32768 + w * 4096 + l * 16;
#pragma unroll
      for (int j = 0; j < 4; ++j) v[i & 1][j] = *(LAS const u4*)(a + j * 1024);
      __builtin_amdgcn_sched_barrier(0);
    }
    if constexpr (i > 0) {
#pragma unroll
      for (int e = 0; e < 4; ++e)
#pragma unroll
        for (int j = 0; j < 4; ++j) {
          const float p = lo16(v[(i - 1) & 1][j][e]), q = hi16(v[(i - 1) & 1][j][e]);
          acc2(sx[j], sq[j], p, q);
        }
    }
  });
#pragma unroll
  for (int j = 0; j < 4; ++j) {
    const float s1 = sum8(sx[j]) * (1.0f / 640.0f), s2 = sum8(sq[j]) * (1.0f / 640.0f);
    if ((l & 7) == 0)
      *(LAS f2*)(lds0 + 131072 + (w * 32 + j * 8 + (l >> 3)) * 8) = f2{s1, __builtin_amdgcn_rsqf(fmaxf(s2 - s1 * s1, 0.f) + 1e-5f)};
  }
  wait_vm_barrier<0>();
}
template <bool SWAP>
DEVI void qkv_body(char* __restrict__ ws, char* smem, int mt, int nt, int w, int l) {
  const int r32 = l & 31, h = l >> 5, wm = w >> 1, wn = w & 1;
  const unsigned lds0 = (unsigned)(uintptr_t)(LAS char*)smem;
  unsigned xa[2][4];
#pragma unroll
  for (int s2 = 0; s2 < 4; ++s2) {
    const unsigned a = lds0 + (wm * 64 + r32) * 128 + (((2 * s2 + h) ^ ((r32 >> 1) & 7)) << 4);
    xa[0][s2] = a + wn * 4096; xa[1][s2] = a + 4096 - wn * 4096;
  }
  const int q4 = (l >> 2) & 3;
  unsigned wa0 = lds0 + 16384 + wn * 8192 + h * 1024 + q4 * 128 + (((q4 >> 1) ^ wm) << 6) + ((l >> 4) & 1) * 32 + (l & 3) * 8;
  unsigned wa1 = wa0 ^ 64u;
  const unsigned selw = ((l & 15) == ((l >> 4) & 1)) ? 0x3f803f80u : 0u;
  const bh8 sel = __builtin_bit_cast(bh8, u4{selw, selw, selw, selw});
  f16v acc[2][2];
#pragma unroll
  for (int i = 0; i < 16; ++i) { acc[0][0][i] = 0.f; acc[0][1][i] = 0.f; acc[1][0][i] = 0.f; acc[1][1][i] = 0.f; }
  f4 csa = {0.f, 0.f, 0.f, 0.f};
  bh8 X[4][2], W[4][2];
  auto rd = [&](auto sc) {
    constexpr int s2 = decltype(sc)::value;
    X[s2][0] = *(LAS const bh8*)(xa[0][s2]);
    W[s2][0] = ldsA(wa0 + s2 * 2048, wa0 + s2 * 2048 + 512);
    W[s2][1] = ldsA(wa1 + s2 * 2048, wa1 + s2 * 2048 + 512);
    X[s2][1] = *(LAS const bh8*)(xa[1][s2]);
  };
  const bool isq = nt < 5;
  const int tstride = isq ? 1280 : KV_TILE;
  unsigned oo[4];
  bool mid[4];
#pragma unroll
  for (int u = 0; u < 4; ++u) {
    const int cb = wn * 64 + ((u >> 1) ^ wm) * 32;
    if (!SWAP) {
      const int G = (isq ? nt : nt - 5) * 16 + (cb >> 3) + 2 * (u & 1) + h, hp = G / 5, e = G - 5 * hp;
      mid[u] = e == 2;
      oo[u] = (unsigned)(hp * 128 * tstride + (e == 2 ? 1024 + r32 * 8 : (e > 2 ? 64 * tstride + (e - 3) * 512 : e * 512) + r32 * 16));
    } else {
      const int G = (nt - 10) * 16 + ((cb + r32) >> 3), hp = G / 5, e = G - 5 * hp, j = r32 & 7;
      const int hd = 2 * hp + (e == 2 ? j >> 2 : e > 2), slot = e == 2 ? 16 + (j & 3) : 8 * (e > 2 ? e - 3 : e) + j;
      mid[u] = e == 2;
      oo[u] = (unsigned)(hd * 64 * KV_TILE + 1280 + (h * 24 + slot) * 16);
    }
    asm volatile("" : "+v"(oo[u]));
  }
  __builtin_amdgcn_s_setprio(2);
  bar();
  rd(IC(0));
  __builtin_amdgcn_sched_barrier(0);
  rd(IC(1));
  static_for<10>([&](auto ic) {
    constexpr int i = decltype(ic)::value;
    constexpr unsigned dn = (i & 3) == 3 ? (unsigned)-98304 : 32768u;
    static_for<4>([&](auto sc) {
      constexpr int s2 = decltype(sc)::value;
      if constexpr (s2 == 2 && i < 9) bar();
      __builtin_amdgcn_sched_barrier(0);
      if constexpr (s2 == 2) { wa0 += dn; wa1 += dn; }
      if constexpr (s2 >= 2) { xa[0][s2 - 2] += dn; xa[1][s2 - 2] += dn; }
      if constexpr (i < 9 || s2 < 2) rd(IC((s2 + 2) & 3));
      if constexpr (s2 < 2) { xa[0][s2 + 2] += dn; xa[1][s2 + 2] += dn; }
#pragma unroll
      for (int t = 0; t < 2; ++t)
#pragma unroll
        for (int n = 0; n < 2; ++n)
          acc[t][n] = SWAP ? mfma32(X[s2][t], W[s2][n], acc[t][n]) : mfma32(W[s2][n], X[s2][t], acc[t][n]);
      csa = mfma16(W[s2][0], sel, csa);
      SGB(0x008, 1); SGB(0x002, 2); SGB(0x100, 2); SGB(0x008, 1); SGB(0x100, 2); SGB(0x002, 2);
      SGB(0x008, 1); SGB(0x100, 2); SGB(0x008, 2);
      asm volatile("" : "+v"(wa0), "+v"(wa1), "+v"(xa[0][s2]), "+v"(xa[1][s2]));
      __builtin_amdgcn_sched_barrier(0);
    });
  });
  f2* stl = (f2*)(smem + 131072);
  float* csl = (float*)(smem + 131072 + 1024);
  {
    if ((l & 15) < 2) *(f4*)(csl + wn * 64 + wm * 32 + (l & 15) * 16 + (l >> 4) * 4) = csa;
  }
  __syncthreads();
  char* tb[2];
#pragma unroll
  for (int t = 0; t < 2; ++t) tb[t] = ws + (isq ? OFF_Q : OFF_KV) + (size_t)(mt * 4 + wm * 2 + (t ^ wn)) * tstride;
  if (!SWAP) {
    const float qs = isq ? 0.22360679774997896f * 1.4426950408889634f : 1.0f;
    float gmv[8];
    const f2 st0 = stl[wm * 64 + wn * 32 + r32], st1 = stl[wm * 64 + (wn ^ 1) * 32 + r32];
    f4 c4[8];
#pragma unroll
    for (int i = 0; i < 8; ++i) c4[i] = *(const f4*)(csl + wn * 64 + ((i >> 2) ^ wm) * 32 + 8 * (i & 3) + 4 * h);
    __builtin_amdgcn_sched_barrier(0);
#pragma unroll
    for (int u = 0; u < 4; ++u) {
      float ga = 0.f, gb = 0.f;
      u4 o[2];
#pragma unroll
      for (int t = 0; t < 2; ++t) {
        const f2 st = t ? st1 : st0;
        const float r = st[1] * qs, mu = st[0];
        float a[4], c[4];
#pragma unroll
        for (int i = 0; i < 4; ++i) {
          a[i] = r * (acc[t][u >> 1][8 * (u & 1) + i] - mu * c4[2 * u][i]);
          c[i] = r * (acc[t][u >> 1][8 * (u & 1) + 4 + i] - mu * c4[2 * u + 1][i]);
        }
        const auto x0 = __builtin_amdgcn_permlane32_swap(pk2(a[0], a[1]), pk2(c[0], c[1]), false, false);
        const auto x1 = __builtin_amdgcn_permlane32_swap(pk2(a[2], a[3]), pk2(c[2], c[3]), false, false);
        o[t] = u4{x0[0], x1[0], x0[1], x1[1]};
        ga = fmaxf(ga, (a[0] * a[0] + a[2] * a[2]) + (a[1] * a[1] + a[3] * a[3]));
        gb = fmaxf(gb, (c[0] * c[0] + c[2] * c[2]) + (c[1] * c[1] + c[3] * c[3]));
      }
      if (!mid[u]) { st16s<0>(tb[0], oo[u], o[0]); st16s<0>(tb[1], oo[u], o[1]); }
      else {
#pragma unroll
        for (int t = 0; t < 2; ++t) { st8s(tb[t], oo[u], u2{o[t][0], o[t][1]}); st8s(tb[t] + 64 * tstride, oo[u], u2{o[t][2], o[t][3]}); }
      }
      gmv[2 * u] = ga; gmv[2 * u + 1] = gb;
    }
    if (!isq) {
      float g = max16(gmv[0]);
#pragma unroll
      for (int i = 1; i < 8; ++i) { const float v = max16(gmv[i]); g = (l & 7) == i ? v : g; }
      if ((l & 15) < 8)
        ((float*)(ws + OFF_GMAX))[((nt - 5) * 32 + wn * 16 + ((((l & 7) >> 2) ^ wm) * 8) + 2 * (l & 3) + h) * 64 + mt * 4 + wm * 2 + ((l >> 4) & 1)] = g;
    }
  } else {
    const float cc0 = csl[wn * 64 + wm * 32 + r32], cc1 = csl[wn * 64 + (wm ^ 1) * 32 + r32];
    const unsigned fill = (r32 & 3) == 0 ? 0x3f803f80u : 0u;
#pragma unroll
    for (int t = 0; t < 2; ++t) {
      f4 sa[4][2];
#pragma unroll
      for (int gi = 0; gi < 4; ++gi) {
        const f4* sp = (const f4*)(stl + wm * 64 + (t ^ wn) * 32 + 8 * gi + 4 * h);
        sa[gi][0] = sp[0]; sa[gi][1] = sp[1];
      }
#pragma unroll
      for (int n = 0; n < 2; ++n) {
        const float cc = n ? cc1 : cc0;
        u4 o[2];
#pragma unroll
        for (int gi = 0; gi < 4; ++gi) {
          o[gi >> 1][2 * (gi & 1)] = pk2(sa[gi][0][1] * (acc[t][n][4 * gi] - sa[gi][0][0] * cc), sa[gi][0][3] * (acc[t][n][4 * gi + 1] - sa[gi][0][2] * cc));
          o[gi >> 1][2 * (gi & 1) + 1] = pk2(sa[gi][1][1] * (acc[t][n][4 * gi + 2] - sa[gi][1][0] * cc), sa[gi][1][3] * (acc[t][n][4 * gi + 3] - sa[gi][1][2] * cc));
        }
        st16s<0>(tb[t], oo[2 * n], o[0]); st16s<768>(tb[t], oo[2 * n], o[1]);
      }
    }
#pragma unroll
    for (int n = 0; n < 2; ++n)
      if (mid[2 * n]) {
#pragma unroll
        for (int t = 0; t < 2; ++t) { st16s<64>(tb[t], oo[2 * n], u4{fill, fill, fill, fill}); st16s<832>(tb[t], oo[2 * n], u4{fill, fill, fill, fill}); }
      }
  }
}

struct Retile { char* dst; int stride; };
DEVI Retile retile_load(int unit, const char* wo, const char* w1, const char* w2, char* ws, unsigned lds, int l) {
  const char* src; int ks, cb, pitch; Retile r;
  if (unit < 200) { ks = unit / 10; cb = unit - ks * 10; src = wo; pitch = 1280; r.dst = ws + OFF_WO; r.stride = 20 * 1024; }
  else if (unit < 680) { const int u = unit - 200; ks = u / 24; cb = u - ks * 24; src = w1; pitch = 3072; r.dst = ws + OFF_W1; r.stride = 20 * 1024; }
  else { const int u = unit - 680; ks = u / 10; cb = u - ks * 10; src = w2; pitch = 1280; r.dst = ws + OFF_W2; r.stride = 48 * 1024; }
  r.dst += (size_t)cb * 4 * r.stride + ks * 1024;
#pragma unroll
  for (int i = 0; i < 4; ++i) {
    const int rho = 8 * i + (l >> 3);
    int k = 32 * ks + rho;
    if (unit < 200) {
      const int U = 4 * ks + ((rho >> 2) & 3), j = 4 * (rho >> 4) + (rho & 3), hp = U / 5, e = U - 5 * hp;
      k = 40 * hp + (e == 2 ? 20 * (j >> 2) + 10 * ((j >> 1) & 1) + 8 + (j & 1) : 10 * (e > 2 ? e - 1 : e) + j);
    }
    else if (unit < 680) {
      const int p = ks / 5, e = ks - 5 * p, g = (rho >> 2) & 3, j = 4 * (rho >> 4) + (rho & 3);
      k = 160 * p + (e == 2 ? 80 * (g >> 1) + 64 + 8 * (g & 1) + j : (e > 2 ? 80 + 32 * (e - 3) : 32 * e) + rho);
    }
    glds16(src + cb * 128, (unsigned)(k * pitch + (l & 7) * 16), lds + i * 1024);
  }
  return r;
}
DEVI void retile_store(Retile r, unsigned lds, int l) {
  const unsigned ta = lds + (l >> 2) * 128 + (l & 3) * 8;
  u4 v[4];
#pragma unroll
  for (int n = 0; n < 4; ++n) v[n] = __builtin_bit_cast(u4, ldsA(ta + n * 32, ta + n * 32 + 2048));
#pragma unroll
  for (int n = 0; n < 4; ++n) st16s<0>(r.dst + (size_t)n * r.stride, (unsigned)(l * 16), v[n]);
}
#define CODE_ADDR(dst, sym) asm volatile("s_getpc_b64 vcc\n\ts_add_u32 vcc_lo, vcc_lo, " sym "@rel32@lo+4\n\ts_addc_u32 vcc_hi, vcc_hi, " sym \
                                         "@rel32@hi+12\n\ts_mov_b64 %0, vcc" : "=s"(dst) :: "vcc", "scc")
DEVI void warm_code(int u) {
  uint64_t a0, a1;
  CODE_ADDR(a0, "k_qkv"); CODE_ADDR(a1, "k_tail");
  constexpr int n0 = CL0, n1 = n0 + CL1;
  if (u < n1) {
    const uint64_t a = u < n0 ? a0 + u * 128 : a1 + (u - n0) * 128;
    unsigned d;
    asm volatile("global_load_dword %0, %1, off\n\ts_waitcnt vmcnt(0)" : "=v"(d) : "v"(a) : "memory");
  }
}
extern "C" __global__ void __launch_bounds__(768, 1) k_qkv(const char* __restrict__ x, const char* __restrict__ wqkv, char* __restrict__ ws) {
  extern __shared__ __attribute__((aligned(16))) char smem[];
  const int b = blockIdx.x, xcd = b & 7, i5 = b >> 3;
  const int mt = (xcd >> 1) * 4 + (i5 & 3), nt = (xcd & 1) * 8 + (i5 >> 2);
  const int l = threadIdx.x & 63, w = uni(threadIdx.x >> 6);
  if (w >= 8 && l < 4) warm_code((i5 * 4 + w - 8) * 4 + l);
  if (nt >= 15) {
    if (threadIdx.x < 3) *(unsigned*)(ws + OFF_CNT + (mt * 3 + threadIdx.x) * 256) = 0u;
    return;
  }
  if (w >= 8) qkv_stats((unsigned)(uintptr_t)(LAS char*)smem, w - 8, l);
  else if (w >= 4) qkv_loader(x, wqkv, (unsigned)(uintptr_t)(LAS char*)smem, mt, nt, w - 4, l);
  else if (nt >= 10) qkv_body<true>(ws, smem, mt, nt, w, l);
  else qkv_body<false>(ws, smem, mt, nt, w, l);
}

DEVI f16v mfma32k8(u2 a, u2 b, f16v c) {
  return __builtin_amdgcn_mfma_f32_32x32x8bf16_1k(__builtin_bit_cast(s4v, a), __builtin_bit_cast(s4v, b), c, 0, 0, 0);
}
DEVI float ex2(float x) { return __builtin_amdgcn_exp2f(x); }
constexpr int AT_CHUNK = 8 * KV_TILE;
constexpr int AT_LDS = 4 * AT_CHUNK + 5 * 4096;
struct KFrag { bh8 lo; u2 hi; };
struct VFrag { bh8 v0, v1; };

DEVI void arrive(char* cnt, int l) {
  if (l == 0) (void)__hip_atomic_fetch_add((unsigned*)cnt, 1u, __ATOMIC_RELAXED, __HIP_MEMORY_SCOPE_AGENT);
}
DEVI void await(char* cnt, int n) {
#pragma unroll 1
  for (int it = 0; it < (1 << 16); ++it) {
    if (uni((int)__hip_atomic_load((unsigned*)cnt, __ATOMIC_RELAXED, __HIP_MEMORY_SCOPE_AGENT)) >= n) break;
    __builtin_amdgcn_s_sleep(1);
  }
}
DEVI void attn_part(char* ws, const char* wo, const char* w1, const char* w2, char* smem) {
  const int b = blockIdx.x, xcd = b & 7, i5 = b >> 3;
  const int head = xcd * 4 + (i5 >> 3), qt = i5 & 7;
  const int l = threadIdx.x & 63, w = uni(threadIdx.x >> 6);
  const int q = l & 31, h = l >> 5;
  const unsigned lds0 = (unsigned)(uintptr_t)(LAS char*)smem;
  const char* kvb = ws + OFF_KV + (size_t)head * 64 * KV_TILE;
  const unsigned lv = l * 16;
  const int p2 = w < 6 ? w + 16 : w + 8;
  auto stage = [&](int c) {
    const char* s_ = kvb + (size_t)c * AT_CHUNK;
    const unsigned d_ = lds0 + (c & 3) * AT_CHUNK;
    glds16(s_ + w * 1024, lv, d_ + w * 1024);
    glds16(s_ + (w + 8) * 1024, lv, d_ + (w + 8) * 1024);
    glds16(s_ + p2 * 1024, lv, d_ + p2 * 1024);
  };
  const int qb32 = qt * 8 + w;
  u2 qa, qb, q1;
  unsigned gm[5];
  stage(0);
  {
    const char* qp = ws + OFF_Q + (size_t)(head * 64 + qb32) * 1280;
    gload8<0>(qa, qp, (unsigned)(h * 512 + q * 16));
    gload8<8>(qb, qp, (unsigned)(h * 512 + q * 16));
    gload8<1024>(q1, qp, (unsigned)(q * 8));
    const char* gp = ws + OFF_GMAX + head * 1280;
    const unsigned gv = (unsigned)(l * 4);
    gload4<0>(gm[0], gp, gv); gload4<256>(gm[1], gp, gv); gload4<512>(gm[2], gp, gv); gload4<768>(gm[3], gp, gv); gload4<1024>(gm[4], gp, gv);
  }
  stage(1);
  const int unit = w * 256 + b;
  const unsigned sj = lds0 + 4 * AT_CHUNK + w * 4096;
  Retile rt = {nullptr, 0};
  char* wcnt = ws + OFF_CNT + 40 * 256;
  auto side_load = [&]() { if (unit < 1160) rt = retile_load(unit, wo, w1, w2, ws, sj, l); };
  asm volatile("s_waitcnt vmcnt(3)" : "+v"(qa), "+v"(qb), "+v"(q1), "+v"(gm[0]), "+v"(gm[1]), "+v"(gm[2]), "+v"(gm[3]), "+v"(gm[4]));
  auto side_job = [&]() { if (unit < 1160) retile_store(rt, sj, l); };
  float gx[5];
#pragma unroll
  for (int i = 0; i < 5; ++i) {
    const int v = (int)__builtin_bit_cast(unsigned, max16(asf(gm[i])));
    gx[i] = fmaxf(fmaxf(asf(__builtin_amdgcn_readlane(v, 0)), asf(__builtin_amdgcn_readlane(v, 16))),
                  fmaxf(asf(__builtin_amdgcn_readlane(v, 32)), asf(__builtin_amdgcn_readlane(v, 48))));
  }
  if (head & 1) { const float g0 = gx[0]; gx[0] = gx[1]; gx[1] = gx[2]; gx[2] = gx[3]; gx[3] = gx[4]; gx[4] = g0; }
  if (h) q1 = u2{0u, 0u};
  auto n4 = [](u2 v) { float a = lo16(v[0]), b2 = hi16(v[0]), c = lo16(v[1]), d = hi16(v[1]); return a * a + b2 * b2 + c * c + d * d; };
  float bound = __builtin_amdgcn_sqrtf(n4(qa) * (h ? gx[2] : gx[0])) + __builtin_amdgcn_sqrtf(n4(qb) * (h ? gx[3] : gx[1])) +
                __builtin_amdgcn_sqrtf(n4(q1) * gx[4]);
  bound = (bound + __shfl_xor(bound, 32)) * 1.01f + 0.01f;
  const bool safe = __all(bound <= 75.0f);
  const bh8 q0 = __builtin_bit_cast(bh8, u4{qa[0], qa[1], qb[0], qb[1]});

  const unsigned rk = lds0 + h * 512 + q * 16, rh = lds0 + 1024 + q * 8;
  const int vi = (q & 3) + 4 * (q >> 3), vd = 10 * ((q >> 2) & 1) + vi;
  const int vslot = vi < 10 ? ((head & 1) ? (vd < 4 ? vd + 16 : vd - 4) : vd) : vi == 10 ? 20 : 21;
  const unsigned rv = lds0 + 1280 + (h * 24 + vslot) * 16;
  auto ldk = [&](unsigned off) { return KFrag{*(LAS const bh8*)(rk + off), *(LAS const u2*)(rh + off)}; };
  auto ldv = [&](unsigned off) { return VFrag{*(LAS const bh8*)(rv + off), *(LAS const bh8*)(rv + off + 768)}; };
  f16v o;
#pragma unroll
  for (int i = 0; i < 16; ++i) o[i] = 0.f;
  bar();
  stage(2);
  if (safe) {
    f16v negm;
#pragma unroll
    for (int i = 0; i < 16; ++i) negm[i] = 50.0f - bound;
    KFrag kn = ldk(0);
    f16v s = mfma32k8(kn.hi, q1, mfma32(kn.lo, q0, negm));
    kn = ldk(KV_TILE);
    VFrag vc = ldv(0);
    bh8 vprev = vc.v1;
    u4 pprev = {0u, 0u, 0u, 0u};
    unsigned ck = rk, ch = rh, cv = rv;
#define FENCE __builtin_amdgcn_sched_barrier(0)
#pragma unroll 1
    for (int c = 0; c < 8; ++c) {
      const unsigned dn = (c & 3) == 3 ? (unsigned)(-3 * AT_CHUNK) : (unsigned)AT_CHUNK;
      if (c == 3) side_job();
      static_for<8>([&](auto tc) {
        constexpr int t = decltype(tc)::value;
        if constexpr (t == 6) {
          if (c < 6) { wait_vm<3>(); bar(); if (c == 0) side_load(); if (c < 5) stage(c + 3); }
          else if (c == 6) { wait_vm<0>(); bar(); if (w == 0) arrive(wcnt, l); }
          ck += dn; ch += dn;
        }
        if constexpr (t == 7) cv += dn;
        asm volatile("" : "+v"(ck), "+v"(ch), "+v"(cv));
        FENCE;
        f16v sn_ = mfma32(kn.lo, q0, negm);
        const bh8 klo = *(LAS const bh8*)(ck + ((t + 2) & 7) * KV_TILE);
        FENCE;
        const float e0 = ex2(s[0]), e1 = ex2(s[1]), e2 = ex2(s[2]), e3 = ex2(s[3]);
        FENCE;
        o = mfma32(vprev, __builtin_bit_cast(bh8, pprev), o);
        const bh8 vn0 = *(LAS const bh8*)(cv + ((t + 1) & 7) * KV_TILE);
        FENCE;
        const u4 p0 = {pk2(e0, e1), pk2(e2, e3), pk2(ex2(s[4]), ex2(s[5])), pk2(ex2(s[6]), ex2(s[7]))};
        FENCE;
        sn_ = mfma32k8(kn.hi, q1, sn_);
        const u2 khi = *(LAS const u2*)(ch + ((t + 2) & 7) * KV_TILE);
        FENCE;
        const float e8 = ex2(s[8]), e9 = ex2(s[9]), e10 = ex2(s[10]), e11 = ex2(s[11]);
        FENCE;
        o = mfma32(vc.v0, __builtin_bit_cast(bh8, p0), o);
        const bh8 vn1 = *(LAS const bh8*)(cv + ((t + 1) & 7) * KV_TILE + 768);
        FENCE;
        pprev = u4{pk2(e8, e9), pk2(e10, e11), pk2(ex2(s[12]), ex2(s[13])), pk2(ex2(s[14]), ex2(s[15]))};
        FENCE;
        s = sn_; kn = KFrag{klo, khi}; vprev = vc.v1; vc = VFrag{vn0, vn1};
      });
    }
    o = mfma32(vprev, __builtin_bit_cast(bh8, pprev), o);
  } else {
    float mrun = -3.0e38f;
    f16v zero;
#pragma unroll
    for (int i = 0; i < 16; ++i) zero[i] = 0.f;
#pragma unroll 1
    for (int c = 0; c < 8; ++c) {
      const unsigned so = (c & 3) * AT_CHUNK;
      if (c == 3) side_job();
#pragma unroll 1
      for (int t = 0; t < 8; ++t) {
        const KFrag kc = ldk(so + t * KV_TILE);
        const VFrag vc = ldv(so + t * KV_TILE);
        f16v s = mfma32k8(kc.hi, q1, mfma32(kc.lo, q0, zero));
        float mx = s[0];
#pragma unroll
        for (int i = 1; i < 16; ++i) mx = fmaxf(mx, s[i]);
        mx = fmaxf(fmaxf(mx, __shfl_xor(mx, 32)), mrun);
        const float alpha = ex2(mrun - mx);
        mrun = mx;
        u4 p0, p1;
#pragma unroll
        for (int i = 0; i < 4; ++i) {
          p0[i] = pk2(ex2(s[2 * i] - mx), ex2(s[2 * i + 1] - mx));
          p1[i] = pk2(ex2(s[8 + 2 * i] - mx), ex2(s[9 + 2 * i] - mx));
        }
#pragma unroll
        for (int i = 0; i < 16; ++i) o[i] *= alpha;
        o = mfma32(vc.v0, __builtin_bit_cast(bh8, p0), o);
        o = mfma32(vc.v1, __builtin_bit_cast(bh8, p1), o);
      }
      if (c < 6) { wait_vm_barrier<3>(); if (c == 0) side_load(); if (c < 5) stage(c + 3); }
      else if (c == 6) { wait_vm_barrier<0>(); if (w == 0) arrive(wcnt, l); }
    }
  }
  const float inv = __builtin_amdgcn_rcpf(o[10]);
  const int tok = qb32 * 32 + q;
  char* ob = ws + OFF_O + (size_t)(tok >> 4) * 20480 + (tok & 15) * 16;
  const int U = 5 * (head >> 1) + 3 * (head & 1) + h, Uc = 5 * (head >> 1) + 2;
  st16(ob + U * 256, u4{pk2(o[0] * inv, o[1] * inv), pk2(o[2] * inv, o[3] * inv), pk2(o[4] * inv, o[5] * inv), pk2(o[6] * inv, o[7] * inv)});
  const unsigned tail = pk2(o[8] * inv, o[9] * inv), tail2 = (unsigned)__shfl_xor((int)tail, 32);
  if (h == 0) st8(ob + Uc * 256 + (head & 1) * 8, u2{tail, tail2});
  if (w == 4) await(wcnt, 256);
  wait_vm_barrier<0>();
  if (w == 4) arrive(ws + OFF_CNT + (32 + qt) * 256, l);
}

static_assert(AT_LDS <= 98304 + 61440, "");
constexpr int T_EX = 98304, T_CS = T_EX + 61440, TAIL_LDS = T_CS + 4 * 96 * 4;
template <bool SC1>
DEVI void ld16(bh8& dst, const void* sbase, unsigned voff) {
  if (SC1) asm volatile("global_load_dwordx4 %0, %1, %2 sc1" : "=v"(dst) : "v"(voff), "s"(sbase) : "memory");
  else asm volatile("global_load_dwordx4 %0, %1, %2" : "=v"(dst) : "v"(voff), "s"(sbase) : "memory");
}
template <int IMM>
DEVI void ld8c(u2& dst, const void* sbase, unsigned voff) {
  asm volatile("global_load_dwordx2 %0, %1, %2 offset:%3 sc1" : "=v"(dst) : "v"(voff), "s"(sbase), "i"(IMM) : "memory");
}
DEVI f4 unpack4(u2 v) { return f4{lo16(v[0]), hi16(v[0]), lo16(v[1]), hi16(v[1])}; }
template <int NI, int RING, class Pre>
DEVI void gemm80(const char* wb, const char* ab, char* smem, int w, int l, f4 (&r)[5], Pre pre) {
  constexpr int KS = 4 * NI;
  unsigned wv[5], av[4];
#pragma unroll
  for (int n = 0; n < 5; ++n) wv[n] = (unsigned)(l * 16 + n * KS * 1024);
#pragma unroll
  for (int t = 0; t < 4; ++t) av[t] = (unsigned)(l * 16 + ((t + w) & 3) * KS * 1024);
  bh8 Wf[RING][5], Af[RING][4];
  auto ldw = [&](auto ic) {
    constexpr int i = decltype(ic)::value;
#pragma unroll
    for (int n = 0; n < 5; ++n) ld16<false>(Wf[i % RING][n], wb + i * 4096, wv[n]);
  };
  auto lda = [&](auto ic) {
    constexpr int i = decltype(ic)::value;
#pragma unroll
    for (int t = 0; t < 4; ++t) ld16<true>(Af[i % RING][t], ab + i * 4096, av[t]);
  };
  static_for<RING>([&](auto ic) { ldw(ic); });
  pre();
  static_for<RING>([&](auto ic) { lda(ic); });
  f4 acc[4][5];
#pragma unroll
  for (int t = 0; t < 4; ++t)
#pragma unroll
    for (int n = 0; n < 5; ++n) acc[t][n] = f4{0.f, 0.f, 0.f, 0.f};
  static_for<NI>([&](auto ic) {
    constexpr int i = decltype(ic)::value, s = i % RING;
    constexpr int later = (NI - 1 - i < RING - 1) ? NI - 1 - i : RING - 1;
    wait_vm<(i < RING) ? 4 * (RING - 1 - i) + 9 * (i < NI - RING ? i : NI - RING) : later * 9>();
    __builtin_amdgcn_sched_barrier(0);
#pragma unroll
    for (int n = 0; n < 5; ++n) pin(Wf[s][n]);
#pragma unroll
    for (int t = 0; t < 4; ++t) pin(Af[s][t]);
#pragma unroll
    for (int t = 0; t < 4; ++t)
#pragma unroll
      for (int n = 0; n < 5; ++n) acc[t][n] = mfma16(Wf[s][n], Af[s][t], acc[t][n]);
    __builtin_amdgcn_sched_barrier(0);
    if constexpr (i + RING < NI) { lda(IC(i + RING)); ldw(IC(i + RING)); }
  });
  f4* ex = (f4*)(smem + T_EX);
#pragma unroll
  for (int t = 1; t < 4; ++t)
#pragma unroll
    for (int n = 0; n < 5; ++n) ex[((((t + w) & 3) * 3 + t - 1) * 5 + n) * 64 + l] = acc[t][n];
  asm volatile("s_waitcnt lgkmcnt(0)\n\ts_barrier" ::: "memory");
#pragma unroll
  for (int n = 0; n < 5; ++n)
    r[n] = (acc[0][n] + ex[((w * 3) * 5 + n) * 64 + l]) + (ex[((w * 3 + 1) * 5 + n) * 64 + l] + ex[((w * 3 + 2) * 5 + n) * 64 + l]);
}

extern "C" __global__ void __launch_bounds__(512, 1) k_tail(const char* x, const char* b1, const char* b2, char* ws, char* out,
                                                            const char* wo, const char* w1, const char* w2) {
  extern __shared__ __attribute__((aligned(16))) char smem[];
  attn_part(ws, wo, w1, w2, smem);
  const int b = blockIdx.x, xcd = b & 7, i5 = b >> 3;
  const int grp = (xcd >> 1) * 4 + (i5 & 3), j = (xcd & 1) * 8 + (i5 >> 2);
  const int mt = grp * 2 + (j & 1), nt = j >> 1;
  const int l = threadIdx.x & 63, w8 = uni(threadIdx.x >> 6), w = w8 & 3;
  const unsigned lds0 = (unsigned)(uintptr_t)(LAS char*)smem, lv = l * 16;
  char* cnt = ws + OFF_CNT + grp * 512;
  if (w8 >= 4) {
    const char* wp = ws + OFF_W1 + (size_t)j * 6 * 20480 + w * 1024;
    auto w1chunk = [&](int c) {
#pragma unroll
      for (int i = 0; i < 6; ++i) glds16(wp + c * 4096 + i * 20480, lv, lds0 + c * 24576 + w * 6144 + i * 1024);
    };
    if (w == 0) {
      await(ws + OFF_CNT + (32 + (grp >> 1)) * 256, 32);
      bar();
    }
#pragma unroll 1
    for (int c = 0; c < 4; ++c) w1chunk(c);
    if (w != 0) bar();
    bar();
    bar();
    if (w == 0) arrive(cnt, l);
    w1chunk(4);
    if (w == 0) await(cnt, 16);
    wait_vm_barrier<0>();
    bar();
#pragma unroll 1
    for (int i = w; i < 30; i += 4) glds16(ws + OFF_W2 + (size_t)(nt * 240 + (i5 & 7) * 30 + i) * 1024, lv, lds0 + w * 1024);
    bar();
    if (w == 0) { arrive(cnt + 256, l); await(cnt + 256, 16); }
    wait_vm_barrier<0>();
    return;
  }
  const int m = l & 15, g = l >> 4, tbe = mt * 4 + w;
  u2 res[5];
  {
    const char* xb = x + (size_t)tbe * 16 * 1280 + nt * 160;
    const unsigned xv = m * 1280 + g * 8;
    gload8<0>(res[0], xb, xv); gload8<32>(res[1], xb, xv); gload8<64>(res[2], xb, xv); gload8<96>(res[3], xb, xv); gload8<128>(res[4], xb, xv);
    f4 r[5];
    gemm80<5, 4>(ws + OFF_WO + (size_t)nt * 5 * 20480 + w * 1024, ws + OFF_O + (size_t)mt * 4 * 20480 + w * 1024, smem, w, l, r,
                        [&]() { bar(); });
    float s1 = 0.f, s2 = 0.f;
#pragma unroll
    for (int n = 0; n < 5; ++n) {
      pin(res[n]);
      const f4 v = r[n] + unpack4(res[n]);
      res[n] = u2{pk2(v[0], v[1]), pk2(v[2], v[3])};
      const f4 a = unpack4(res[n]);
      s1 += (a[0] + a[1]) + (a[2] + a[3]);
      s2 += a[0] * a[0] + a[1] * a[1] + a[2] * a[2] + a[3] * a[3];
    }
    char* hb = ws + OFF_H + (size_t)tbe * 20480 + (nt >> 1) * 5120;
    st16s<0>(hb + (nt & 1) * 3072, lv, u4{res[0][0], res[0][1], res[1][0], res[1][1]});
    st16s<1024>(hb + (nt & 1) * 3072, lv, u4{res[2][0], res[2][1], res[3][0], res[3][1]});
    const auto y0 = __builtin_amdgcn_permlane16_swap(res[4][0], res[4][0], false, false);
    const auto y1 = __builtin_amdgcn_permlane16_swap(res[4][1], res[4][1], false, false);
    if ((g & 1) == 0) st16s<2048>(hb + (nt & 1) * 512, (unsigned)((g >> 1) * 256 + m * 16), u4{y0[0], y1[0], y0[1], y1[1]});
    s1 = xsum_g(s1); s2 = xsum_g(s2);
    if (g == 0) st8s(ws + OFF_ST + (size_t)(nt * 2048 + tbe * 16) * 8, (unsigned)(m * 8), u2{__builtin_bit_cast(unsigned, s1), __builtin_bit_cast(unsigned, s2)});
    wait_vm_barrier<0>();
  }
  {
    const int tb0 = grp * 8 + w * 2;
    const char* a0 = ws + OFF_H + (size_t)tb0 * 20480;
    unsigned av[4], ra[4];
#pragma unroll
    for (int q = 0; q < 4; ++q) { av[q] = lv + ((q + w) & 3) * 1024; ra[q] = lds0 + lv + ((q + w) & 3) * 6144; }
    u2 st[2][2], bb[6];
    {
      const char* bp = b1 + j * 192;
      const unsigned bv = g * 8;
      gload8<0>(bb[0], bp, bv); gload8<32>(bb[1], bp, bv); gload8<64>(bb[2], bp, bv);
      gload8<96>(bb[3], bp, bv); gload8<128>(bb[4], bp, bv); gload8<160>(bb[5], bp, bv);
    }
    f4 acc[2][6], cs[6];
#pragma unroll
    for (int n = 0; n < 6; ++n) { acc[0][n] = f4{0.f, 0.f, 0.f, 0.f}; acc[1][n] = f4{0.f, 0.f, 0.f, 0.f}; cs[n] = f4{0.f, 0.f, 0.f, 0.f}; }
    const bh8 ones = __builtin_bit_cast(bh8, u4{0x3f803f80u, 0x3f803f80u, 0x3f803f80u, 0x3f803f80u});
    bh8 A[2][6], act[9][2];
    auto lda = [&](auto kc) {
      constexpr int ks = decltype(kc)::value;
      ld16<true>(act[ks % 9][0], a0 + (ks >> 2) * 4096, av[ks & 3]);
      ld16<true>(act[ks % 9][1], a0 + (ks >> 2) * 4096 + 20480, av[ks & 3]);
    };
    auto rd = [&](auto kc) {
      constexpr int ks = decltype(kc)::value;
#pragma unroll
      for (int n = 0; n < 6; ++n) A[ks & 1][n] = *(LAS const bh8*)(ra[ks & 3] + (ks >> 2) * 24576 + n * 1024);
    };
    bar();
    static_for<2>([&](auto kc) { lda(kc); });
    {
      const char* sb = ws + OFF_ST + (size_t)tb0 * 16 * 8;
      const unsigned sv = (unsigned)(g * 2048 + m) * 8;
      ld8c<0>(st[0][0], sb, sv); ld8c<128>(st[1][0], sb, sv);
      ld8c<0>(st[0][1], sb + 4 * 2048 * 8, sv); ld8c<128>(st[1][1], sb + 4 * 2048 * 8, sv);
    }
    static_for<6>([&](auto kc) { lda(IC(decltype(kc)::value + 2)); });
    rd(IC(0));
    static_for<20>([&](auto kc) {
      constexpr int ks = decltype(kc)::value;
      wait_vm<2 * ((ks + 7 < 19 ? ks + 7 : 19) - ks) + (ks < 2 ? 4 : 0)>();
      __builtin_amdgcn_sched_barrier(0);
      pin(act[ks % 9][0]); pin(act[ks % 9][1]);
      if constexpr (ks < 19) rd(IC(ks + 1));
#pragma unroll
      for (int n = 0; n < 6; ++n) {
        acc[0][n] = mfma16(A[ks & 1][n], act[ks % 9][0], acc[0][n]);
        acc[1][n] = mfma16(A[ks & 1][n], act[ks % 9][1], acc[1][n]);
        if constexpr ((ks & 3) == 0) cs[n] = mfma16(A[ks & 1][n], ones, cs[n]);
      }
#pragma unroll
      for (int i = 0; i < 6; ++i) { SGB(0x008, 1); SGB(0x100, 1); SGB(0x008, 2); }
      __builtin_amdgcn_sched_barrier(0);
      if constexpr (ks + 8 < 20) lda(IC(ks + 8));
    });
    float* csl = (float*)(smem + T_CS);
    if (m == 0) {
#pragma unroll
      for (int n = 0; n < 6; ++n) *(f4*)(csl + w * 96 + n * 16 + 4 * g) = cs[n];
    }
    float mean[2], rstd[2];
#pragma unroll
    for (int t = 0; t < 2; ++t) {
      pin(st[t][0]); pin(st[t][1]);
      const float s = xsum_g(asf(st[t][0][0]) + asf(st[t][1][0])) * (1.0f / 640.0f);
      const float q = xsum_g(asf(st[t][0][1]) + asf(st[t][1][1])) * (1.0f / 640.0f);
      mean[t] = s;
      rstd[t] = __builtin_amdgcn_rsqf(fmaxf(q - s * s, 0.f) + 1e-5f);
    }
    asm volatile("s_waitcnt lgkmcnt(0)\n\ts_barrier" ::: "memory");
    char* hb = ws + OFF_H1 + (size_t)(tb0 * 48 + j * 3) * 1024;
    f4 c4[6];
#pragma unroll
    for (int n = 0; n < 6; ++n) {
      const float* cp = csl + n * 16 + 4 * g;
      c4[n] = (*(const f4*)cp + *(const f4*)(cp + 96)) + (*(const f4*)(cp + 192) + *(const f4*)(cp + 288));
    }
    static_for<3>([&](auto pc) {
      constexpr int p = decltype(pc)::value;
      u4 o[2];
#pragma unroll
      for (int e = 0; e < 2; ++e) {
        const int n = 2 * p + e;
        pin(bb[n]);
        const f4 bv = unpack4(bb[n]);
#pragma unroll
        for (int t = 0; t < 2; ++t) {
          float z[4];
#pragma unroll
          for (int r = 0; r < 4; ++r) {
            const float y = fmaxf(rstd[t] * (acc[t][n][r] - mean[t] * c4[n][r]) + bv[r], 0.f);
            z[r] = y * y;
          }
          o[t][2 * e] = pk2(z[0], z[1]);
          o[t][2 * e + 1] = pk2(z[2], z[3]);
        }
      }
      st16s<p * 1024>(hb, lv, o[0]);
      st16s<p * 1024>(hb + 49152, lv, o[1]);
    });
    wait_vm_barrier<0>();
  }
  {
    u2 bb[5];
    const char* bp = b2 + nt * 160;
    const unsigned bv = g * 8;
    gload8<0>(bb[0], bp, bv); gload8<32>(bb[1], bp, bv); gload8<64>(bb[2], bp, bv); gload8<96>(bb[3], bp, bv); gload8<128>(bb[4], bp, bv);
    f4 r[5];
    gemm80<12, 3>(ws + OFF_W2 + (size_t)nt * 5 * 49152 + w * 1024, ws + OFF_H1 + (size_t)mt * 4 * 49152 + w * 1024, smem, w, l, r,
                        [&]() { bar(); });
#pragma unroll
    for (int n = 0; n < 5; ++n) {
      pin(bb[n]);
      const f4 v = (r[n] + unpack4(res[n])) + unpack4(bb[n]);
      *(u2*)(out + (size_t)(tbe * 16 + m) * 1280 + nt * 160 + n * 32 + g * 8) = u2{pk2(v[0], v[1]), pk2(v[2], v[3])};
    }
  }
}

extern "C" void kernel_launch(void* const* d_in, const int* in_sizes, int n_in, void* d_out, int out_size, void* d_ws,
                              size_t ws_size, hipStream_t stream) {
  const char* x = (const char*)d_in[0];
  const char* wqkv = (const char*)d_in[1];
  const char* wo = (const char*)d_in[2];
  const char* w1 = (const char*)d_in[4];
  const char* b1 = (const char*)d_in[5];
  const char* w2 = (const char*)d_in[6];
  const char* b2 = (const char*)d_in[7];
  char* ws = (char*)d_ws;
  (void)hipFuncSetAttribute((const void*)k_qkv, hipFuncAttributeMaxDynamicSharedMemorySize, K1_LDS);
  (void)hipFuncSetAttribute((const void*)k_tail, hipFuncAttributeMaxDynamicSharedMemorySize, TAIL_LDS);
  k_qkv<<<256, 768, K1_LDS, stream>>>(x, wqkv, ws);
  k_tail<<<256, 512, TAIL_LDS, stream>>>(x, b1, b2, ws, (char*)d_out, wo, w1, w2);
}
```
